```python
import torch

def _softmax(x, axis=-1):
    return torch.softmax(x, dim=axis)
def _gelu(x):
    return torch.nn.functional.gelu(x, approximate="tanh")
def _silu(x):
    return torch.nn.functional.silu(x)
def _sigmoid(x):
    return torch.sigmoid(x)
def _layernorm(x, axis=-1, eps=1e-5):
    m = x.mean(dim=axis, keepdim=True)
    v = x.var(dim=axis, keepdim=True, unbiased=False)
    return (x - m) / torch.sqrt(v + eps)
def _scatter_add(tgt, idx, src, axis=0):
    return tgt.clone().index_add_(axis, idx, src)
def _segment_sum(src, seg, n):
    out = torch.zeros((n,) + src.shape[1:], dtype=src.dtype)
    return out.index_add_(0, seg, src)
def _pad(x, pw):
    flat = tuple(v for pair in reversed(pw) for v in pair)
    return torch.nn.functional.pad(x, flat)

kernel_name = 'standalone__channel_attn_se__vision'

def setup_inputs():

    _w = (lambda a: (lambda t: t.to(torch.bfloat16) if t.is_floating_point() else t)(torch.as_tensor(a)))
    H = W = 512
    C = 96
    r = max(C // 16, 4)
    return {
        "x": _w((lambda *s: torch.randn(*s, dtype=torch.bfloat16) * 3.2)(1, H, W, C)),
        "w_down": _w((lambda *s: torch.randn(*s, dtype=torch.bfloat16) * 12.8)(C, r)),
        "w_up": _w((lambda *s: torch.randn(*s, dtype=torch.bfloat16) * 3.2)(r, C)),
    }

def reference(x, w_down, w_up):
    s = x.mean(axis=(1, 2))
    h = _silu(torch.einsum('bc,cr->br', s, w_down))
    g = _sigmoid(torch.einsum('br,rc->bc', h, w_up))
    return x * g[:, None, None, :]
```

```cpp
#include <hip/hip_runtime.h>
#include <hip/hip_bf16.h>
#include <stdint.h>

#define C 96
#define R 6
#define NPIX (512*512)
#define NWG 256
#define NT 768
#define PER 16

#define GAS __attribute__((address_space(1)))
typedef GAS unsigned long long gu64;
#define RLX_AGENT __ATOMIC_RELAXED, __HIP_MEMORY_SCOPE_AGENT

__device__ __forceinline__ float bf_lo(uint32_t w) { return __uint_as_float(w << 16); }
__device__ __forceinline__ float bf_hi(uint32_t w) { return __uint_as_float(w & 0xffff0000u); }
__device__ __forceinline__ float rnd_bf16(float f) { return __bfloat162float(__float2bfloat16(f)); }
__device__ __forceinline__ uint32_t pack_bf16(float lo, float hi) {
    uint32_t a = (uint32_t)__bfloat16_as_ushort(__float2bfloat16(lo));
    uint32_t b = (uint32_t)__bfloat16_as_ushort(__float2bfloat16(hi));
    return a | (b << 16);
}
__device__ __forceinline__ float silu_f(float v) { return v / (1.f + __expf(-v)); }
__device__ __forceinline__ float sigm_f(float v) { return 1.f / (1.f + __expf(-v)); }

#define SPIN_CAP (1u << 20)

__global__ void __launch_bounds__(NT, 3) se_fused(const uint4* __restrict__ x, const __hip_bfloat16* __restrict__ wd,
                                                  const __hip_bfloat16* __restrict__ wu, uint4* __restrict__ out,
                                                  unsigned long long* ws) {
    __shared__ float red[64 * C];
    __shared__ float red2[8 * C];
    __shared__ float s_sh[C];
    __shared__ float g_sh[C];
    const int t = threadIdx.x;
    const int wg = blockIdx.x;
    const size_t base = (size_t)wg * (NT * PER);
    gu64* gran1 = (gu64*)ws;
    gu64* gran2 = gran1 + C * NWG;

    uint4 v[PER];
#pragma unroll
    for (int i = 0; i < PER; ++i) v[i] = x[base + i * NT + t];
    float a[8];
#pragma unroll
    for (int j = 0; j < 8; ++j) a[j] = 0.f;
#pragma unroll
    for (int i = 0; i < PER; ++i) {
        a[0] += bf_lo(v[i].x); a[1] += bf_hi(v[i].x);
        a[2] += bf_lo(v[i].y); a[3] += bf_hi(v[i].y);
        a[4] += bf_lo(v[i].z); a[5] += bf_hi(v[i].z);
        a[6] += bf_lo(v[i].w); a[7] += bf_hi(v[i].w);
    }
#pragma unroll
    for (int j = 0; j < 8; ++j) red[t * 8 + j] = a[j];
    __syncthreads();
    {
        const int c = t % C, k = t / C;
        float s = 0.f;
#pragma unroll
        for (int j = 0; j < 8; ++j) s += red[(k * 8 + j) * C + c];
        red2[k * C + c] = s;
    }
    __syncthreads();
    if (t < C) {
        float s = 0.f;
#pragma unroll
        for (int j = 0; j < 8; ++j) s += red2[j * C + t];
        __hip_atomic_store(gran1 + t * NWG + wg, (1ull << 32) | (unsigned long long)__float_as_uint(s), RLX_AGENT);
    }
    if (wg < C && t < 64) {
        gu64* gsrc = gran1 + wg * NWG + t * 4;
        float s;
        for (unsigned spins = 0;; ++spins) {
            unsigned long long q0 = __hip_atomic_load(gsrc + 0, RLX_AGENT);
            unsigned long long q1 = __hip_atomic_load(gsrc + 1, RLX_AGENT);
            unsigned long long q2 = __hip_atomic_load(gsrc + 2, RLX_AGENT);
            unsigned long long q3 = __hip_atomic_load(gsrc + 3, RLX_AGENT);
            bool ok = ((q0 >> 32) == 1ull) & ((q1 >> 32) == 1ull) & ((q2 >> 32) == 1ull) & ((q3 >> 32) == 1ull);
            s = (__uint_as_float((unsigned)q0) + __uint_as_float((unsigned)q1)) +
                (__uint_as_float((unsigned)q2) + __uint_as_float((unsigned)q3));
            if (__all(ok) || spins > SPIN_CAP) break;
            __builtin_amdgcn_s_sleep(1);
        }
#pragma unroll
        for (int m = 32; m >= 1; m >>= 1) s += __shfl_xor(s, m, 64);
        if (t == 0) __hip_atomic_store(gran2 + wg, (1ull << 32) | (unsigned long long)__float_as_uint(s), RLX_AGENT);
    }
    if (t < C) {
        unsigned long long q;
        for (unsigned spins = 0;; ++spins) {
            q = __hip_atomic_load(gran2 + t, RLX_AGENT);
            if ((q >> 32) == 1ull || spins > SPIN_CAP) break;
            __builtin_amdgcn_s_sleep(1);
        }
        s_sh[t] = rnd_bf16(__uint_as_float((unsigned)q) * (1.0f / NPIX));
    }
    __syncthreads();
    if (t < C) {
        float h[R];
#pragma unroll
        for (int r = 0; r < R; ++r) h[r] = 0.f;
        for (int j = 0; j < C; ++j) {
            float sj = s_sh[j];
#pragma unroll
            for (int r = 0; r < R; ++r) h[r] += sj * __bfloat162float(wd[j * R + r]);
        }
        float gsum = 0.f;
#pragma unroll
        for (int r = 0; r < R; ++r) {
            float hv = rnd_bf16(silu_f(rnd_bf16(h[r])));
            gsum += hv * __bfloat162float(wu[r * C + t]);
        }
        g_sh[t] = rnd_bf16(sigm_f(rnd_bf16(gsum)));
    }
    __syncthreads();
    float gg[8];
#pragma unroll
    for (int j = 0; j < 8; ++j) gg[j] = g_sh[(t % 12) * 8 + j];
#pragma unroll
    for (int i = 0; i < PER; ++i) {
        uint4 o;
        o.x = pack_bf16(bf_lo(v[i].x) * gg[0], bf_hi(v[i].x) * gg[1]);
        o.y = pack_bf16(bf_lo(v[i].y) * gg[2], bf_hi(v[i].y) * gg[3]);
        o.z = pack_bf16(bf_lo(v[i].z) * gg[4], bf_hi(v[i].z) * gg[5]);
        o.w = pack_bf16(bf_lo(v[i].w) * gg[6], bf_hi(v[i].w) * gg[7]);
        out[base + i * NT + t] = o;
    }
}

extern "C" void kernel_launch(void* const* d_in, const int* in_sizes, int n_in,
                              void* d_out, int out_size, void* d_ws, size_t ws_size,
                              hipStream_t stream) {
    const uint4* x = (const uint4*)d_in[0];
    const __hip_bfloat16* wd = (const __hip_bfloat16*)d_in[1];
    const __hip_bfloat16* wu = (const __hip_bfloat16*)d_in[2];
    hipMemsetAsync(d_ws, 0, (size_t)(C * NWG + C) * 8, stream);
    se_fused<<<NWG, NT, 0, stream>>>(x, wd, wu, (uint4*)d_out, (unsigned long long*)d_ws);
}
```

```cpp
#include <hip/hip_runtime.h>
#include <hip/hip_bf16.h>
#include <stdint.h>

#define C 96
#define R 6
#define NPIX (512*512)
#define NWG 256
#define NT 768
#define PER 16

#define GAS __attribute__((address_space(1)))
typedef GAS unsigned long long gu64;
#define RLX_AGENT __ATOMIC_RELAXED, __HIP_MEMORY_SCOPE_AGENT

__device__ __forceinline__ float bf_lo(uint32_t w) { return __uint_as_float(w << 16); }
__device__ __forceinline__ float bf_hi(uint32_t w) { return __uint_as_float(w & 0xffff0000u); }
__device__ __forceinline__ float rnd_bf16(float f) { return __bfloat162float(__float2bfloat16(f)); }
__device__ __forceinline__ uint32_t pack_bf16(float lo, float hi) {
    uint32_t a = (uint32_t)__bfloat16_as_ushort(__float2bfloat16(lo));
    uint32_t b = (uint32_t)__bfloat16_as_ushort(__float2bfloat16(hi));
    return a | (b << 16);
}
__device__ __forceinline__ float silu_f(float v) { return v / (1.f + __expf(-v)); }
__device__ __forceinline__ float sigm_f(float v) { return 1.f / (1.f + __expf(-v)); }

#define SPIN_CAP (1u << 20)

#define STAMP(k)

__global__ void __launch_bounds__(NT, 3) se_fused(const uint4* __restrict__ x, const __hip_bfloat16* __restrict__ wd,
                                                  const __hip_bfloat16* __restrict__ wu, uint4* __restrict__ out,
                                                  unsigned long long* ws) {
    __shared__ float red[64 * C];
    __shared__ float red2[8 * C];
    __shared__ float s_sh[C];
    __shared__ float g_sh[C];
    __shared__ float h_sh[8];
    __shared__ float wd_sh[C * R];
    __shared__ float wu_sh[C * R];
    const int t = threadIdx.x;
    const int wg = blockIdx.x;
    const size_t base = (size_t)wg * (NT * PER);
    gu64* gran1 = (gu64*)ws;
    gu64* gran2 = gran1 + C * NWG;
    STAMP(0);

    uint4 v[PER];
#pragma unroll
    for (int i = 0; i < PER; ++i) v[i] = x[base + i * NT + t];
    if (t < C * R) {
        wd_sh[t] = __bfloat162float(wd[t]);
        wu_sh[t] = __bfloat162float(wu[t]);
    }
    float a[8];
#pragma unroll
    for (int j = 0; j < 8; ++j) a[j] = 0.f;
#pragma unroll
    for (int i = 0; i < PER; ++i) {
        a[0] += bf_lo(v[i].x); a[1] += bf_hi(v[i].x);
        a[2] += bf_lo(v[i].y); a[3] += bf_hi(v[i].y);
        a[4] += bf_lo(v[i].z); a[5] += bf_hi(v[i].z);
        a[6] += bf_lo(v[i].w); a[7] += bf_hi(v[i].w);
    }
#pragma unroll
    for (int i = 0; i < PER; ++i) asm volatile("" : "+v"(v[i].x), "+v"(v[i].y), "+v"(v[i].z), "+v"(v[i].w));
#pragma unroll
    for (int j = 0; j < 8; ++j) red[t * 8 + j] = a[j];
    STAMP(1);
    __syncthreads();
    {
        const int c = t % C, k = t / C;
        float s = 0.f;
#pragma unroll
        for (int j = 0; j < 8; ++j) s += red[(k * 8 + j) * C + c];
        red2[k * C + c] = s;
    }
    __syncthreads();
    if (t < C) {
        float s = 0.f;
#pragma unroll
        for (int j = 0; j < 8; ++j) s += red2[j * C + t];
        __hip_atomic_store(gran1 + t * NWG + wg, (1ull << 32) | (unsigned long long)__float_as_uint(s), RLX_AGENT);
    }
    STAMP(2);
    if (wg < C && (t >> 6) == 2) {
        const int l = t & 63;
        gu64* gsrc = gran1 + wg * NWG + l * 4;
        float s;
        for (unsigned spins = 0;; ++spins) {
            unsigned long long q0 = __hip_atomic_load(gsrc + 0, RLX_AGENT);
            unsigned long long q1 = __hip_atomic_load(gsrc + 1, RLX_AGENT);
            unsigned long long q2 = __hip_atomic_load(gsrc + 2, RLX_AGENT);
            unsigned long long q3 = __hip_atomic_load(gsrc + 3, RLX_AGENT);
            bool ok = ((q0 >> 32) == 1ull) & ((q1 >> 32) == 1ull) & ((q2 >> 32) == 1ull) & ((q3 >> 32) == 1ull);
            s = (__uint_as_float((unsigned)q0) + __uint_as_float((unsigned)q1)) +
                (__uint_as_float((unsigned)q2) + __uint_as_float((unsigned)q3));
            if (__all(ok) || spins > SPIN_CAP) break;
            __builtin_amdgcn_s_sleep(1);
        }
#pragma unroll
        for (int m = 32; m >= 1; m >>= 1) s += __shfl_xor(s, m, 64);
        if (l == 0) __hip_atomic_store(gran2 + wg, (1ull << 32) | (unsigned long long)__float_as_uint(s), RLX_AGENT);
    }
    if (t < C) {
        unsigned long long q;
        for (unsigned spins = 0;; ++spins) {
            q = __hip_atomic_load(gran2 + t, RLX_AGENT);
            if ((q >> 32) == 1ull || spins > SPIN_CAP) break;
            __builtin_amdgcn_s_sleep(1);
        }
        s_sh[t] = rnd_bf16(__uint_as_float((unsigned)q) * (1.0f / NPIX));
    }
    __syncthreads();
    STAMP(3);
    if (t < R * 64) {
        const int r = t >> 6, l = t & 63;
        float p = s_sh[l] * wd_sh[l * R + r];
        if (l < C - 64) p += s_sh[l + 64] * wd_sh[(l + 64) * R + r];
#pragma unroll
        for (int m = 32; m >= 1; m >>= 1) p += __shfl_xor(p, m, 64);
        if (l == 0) h_sh[r] = rnd_bf16(silu_f(rnd_bf16(p)));
    }
    __syncthreads();
    if (t < C) {
        float gsum = 0.f;
#pragma unroll
        for (int r = 0; r < R; ++r) gsum += h_sh[r] * wu_sh[r * C + t];
        g_sh[t] = rnd_bf16(sigm_f(rnd_bf16(gsum)));
    }
    __syncthreads();
    STAMP(4);
    float gg[8];
#pragma unroll
    for (int j = 0; j < 8; ++j) gg[j] = g_sh[(t % 12) * 8 + j];
#pragma unroll
    for (int i = 0; i < PER; ++i) {
        uint4 o;
        o.x = pack_bf16(bf_lo(v[i].x) * gg[0], bf_hi(v[i].x) * gg[1]);
        o.y = pack_bf16(bf_lo(v[i].y) * gg[2], bf_hi(v[i].y) * gg[3]);
        o.z = pack_bf16(bf_lo(v[i].z) * gg[4], bf_hi(v[i].z) * gg[5]);
        o.w = pack_bf16(bf_lo(v[i].w) * gg[6], bf_hi(v[i].w) * gg[7]);
        out[base + i * NT + t] = o;
    }
    STAMP(5);
}

extern "C" void kernel_launch(void* const* d_in, const int* in_sizes, int n_in,
                              void* d_out, int out_size, void* d_ws, size_t ws_size,
                              hipStream_t stream) {
    const uint4* x = (const uint4*)d_in[0];
    const __hip_bfloat16* wd = (const __hip_bfloat16*)d_in[1];
    const __hip_bfloat16* wu = (const __hip_bfloat16*)d_in[2];
    (void)hipMemsetAsync(d_ws, 0, (size_t)(C * NWG + C) * 8, stream);
    se_fused<<<NWG, NT, 0, stream>>>(x, wd, wu, (uint4*)d_out, (unsigned long long*)d_ws);
}
```

```cpp
#include <hip/hip_runtime.h>
#include <hip/hip_bf16.h>
#include <stdint.h>

#define C 96
#define R 6
#define NPIX (512*512)
#define NWG 256
#define NT 768
#define PER 16
typedef uint32_t v4u __attribute__((ext_vector_type(4)));

__device__ __forceinline__ float bf_lo(uint32_t w) { return __uint_as_float(w << 16); }
__device__ __forceinline__ float bf_hi(uint32_t w) { return __uint_as_float(w & 0xffff0000u); }
__device__ __forceinline__ float rnd_bf16(float f) { return __bfloat162float(__float2bfloat16(f)); }
__device__ __forceinline__ uint32_t pack_bf16(float lo, float hi) {
    uint32_t a = (uint32_t)__bfloat16_as_ushort(__float2bfloat16(lo));
    uint32_t b = (uint32_t)__bfloat16_as_ushort(__float2bfloat16(hi));
    return a | (b << 16);
}
__device__ __forceinline__ float silu_f(float v) { return v / (1.f + __expf(-v)); }
__device__ __forceinline__ float sigm_f(float v) { return 1.f / (1.f + __expf(-v)); }

#define K1_NTL 0
#define K2_NTL 1
#define K2_NTS 1

__global__ void __launch_bounds__(NT, 3) k_part(const v4u* __restrict__ x, float* __restrict__ part) {
    __shared__ float red[64 * C];
    __shared__ float red2[8 * C];
    const int t = threadIdx.x;
    const int wg = blockIdx.x;
    const size_t base = (size_t)wg * (NT * PER);
    v4u v[PER];
#pragma unroll
    for (int i = 0; i < PER; ++i) v[i] = K1_NTL ? __builtin_nontemporal_load(&x[base + i * NT + t]) : x[base + i * NT + t];
    float a[8];
#pragma unroll
    for (int j = 0; j < 8; ++j) a[j] = 0.f;
#pragma unroll
    for (int i = 0; i < PER; ++i) {
        a[0] += bf_lo(v[i].x); a[1] += bf_hi(v[i].x);
        a[2] += bf_lo(v[i].y); a[3] += bf_hi(v[i].y);
        a[4] += bf_lo(v[i].z); a[5] += bf_hi(v[i].z);
        a[6] += bf_lo(v[i].w); a[7] += bf_hi(v[i].w);
    }
#pragma unroll
    for (int j = 0; j < 8; ++j) red[t * 8 + j] = a[j];
    __syncthreads();
    {
        const int c = t % C, k = t / C;
        float s = 0.f;
#pragma unroll
        for (int j = 0; j < 8; ++j) s += red[(k * 8 + j) * C + c];
        red2[k * C + c] = s;
    }
    __syncthreads();
    if (t < C) {
        float s = 0.f;
#pragma unroll
        for (int j = 0; j < 8; ++j) s += red2[j * C + t];
        part[wg * C + t] = s;
    }
}

__global__ void __launch_bounds__(NT, 3) k_scale(const v4u* __restrict__ x, const __hip_bfloat16* __restrict__ wd,
                                                  const __hip_bfloat16* __restrict__ wu, v4u* __restrict__ out,
                                                  const float* __restrict__ part) {
    __shared__ float red2[8 * C];
    __shared__ float s_sh[C];
    __shared__ float g_sh[C];
    __shared__ float h_sh[8];
    __shared__ float wd_sh[C * R];
    __shared__ float wu_sh[C * R];
    const int t = threadIdx.x;
    const int wg = blockIdx.x;
    const size_t base = (size_t)wg * (NT * PER);
    v4u v[PER];
#pragma unroll
    for (int i = 0; i < PER; ++i) v[i] = K2_NTL ? __builtin_nontemporal_load(&x[base + i * NT + t]) : x[base + i * NT + t];
    if (t < C * R) {
        wd_sh[t] = __bfloat162float(wd[t]);
        wu_sh[t] = __bfloat162float(wu[t]);
    }
    {
        const int c = t % C, k = t / C;
        float s = 0.f;
#pragma unroll
        for (int j = 0; j < NWG / 8; ++j) s += part[(k * (NWG / 8) + j) * C + c];
        red2[k * C + c] = s;
    }
    __syncthreads();
    if (t < C) {
        float s = 0.f;
#pragma unroll
        for (int j = 0; j < 8; ++j) s += red2[j * C + t];
        s_sh[t] = rnd_bf16(s * (1.0f / NPIX));
    }
    __syncthreads();
    if (t < R * 64) {
        const int r = t >> 6, l = t & 63;
        float p = s_sh[l] * wd_sh[l * R + r];
        if (l < C - 64) p += s_sh[l + 64] * wd_sh[(l + 64) * R + r];
#pragma unroll
        for (int m = 32; m >= 1; m >>= 1) p += __shfl_xor(p, m, 64);
        if (l == 0) h_sh[r] = rnd_bf16(silu_f(rnd_bf16(p)));
    }
    __syncthreads();
    if (t < C) {
        float gsum = 0.f;
#pragma unroll
        for (int r = 0; r < R; ++r) gsum += h_sh[r] * wu_sh[r * C + t];
        g_sh[t] = rnd_bf16(sigm_f(rnd_bf16(gsum)));
    }
    __syncthreads();
    float gg[8];
#pragma unroll
    for (int j = 0; j < 8; ++j) gg[j] = g_sh[(t % 12) * 8 + j];
#pragma unroll
    for (int i = 0; i < PER; ++i) {
        v4u o;
        o.x = pack_bf16(bf_lo(v[i].x) * gg[0], bf_hi(v[i].x) * gg[1]);
        o.y = pack_bf16(bf_lo(v[i].y) * gg[2], bf_hi(v[i].y) * gg[3]);
        o.z = pack_bf16(bf_lo(v[i].z) * gg[4], bf_hi(v[i].z) * gg[5]);
        o.w = pack_bf16(bf_lo(v[i].w) * gg[6], bf_hi(v[i].w) * gg[7]);
        if (K2_NTS) __builtin_nontemporal_store(o, &out[base + i * NT + t]); else out[base + i * NT + t] = o;
    }
}

extern "C" void kernel_launch(void* const* d_in, const int* in_sizes, int n_in,
                              void* d_out, int out_size, void* d_ws, size_t ws_size,
                              hipStream_t stream) {
    const v4u* x = (const v4u*)d_in[0];
    const __hip_bfloat16* wd = (const __hip_bfloat16*)d_in[1];
    const __hip_bfloat16* wu = (const __hip_bfloat16*)d_in[2];
    k_part<<<NWG, NT, 0, stream>>>(x, (float*)d_ws);
    k_scale<<<NWG, NT, 0, stream>>>(x, wd, wu, (v4u*)d_out, (const float*)d_ws);
}
```

```cpp
#include <hip/hip_runtime.h>
#include <hip/hip_bf16.h>
#include <stdint.h>
#define STAMPS 1

#define C 96
#define R 6
#define NPIX (512*512)
#define NWG 256
#define NT 768
#define PER 16

typedef uint32_t v4u __attribute__((ext_vector_type(4)));
#define GAS __attribute__((address_space(1)))
typedef GAS unsigned long long gu64;
#define RLX_AGENT __ATOMIC_RELAXED, __HIP_MEMORY_SCOPE_AGENT

__device__ __forceinline__ float bf_lo(uint32_t w) { return __uint_as_float(w << 16); }
__device__ __forceinline__ float bf_hi(uint32_t w) { return __uint_as_float(w & 0xffff0000u); }
__device__ __forceinline__ float rnd_bf16(float f) { return __bfloat162float(__float2bfloat16(f)); }
__device__ __forceinline__ uint32_t pack_bf16(float lo, float hi) {
    uint32_t a = (uint32_t)__bfloat16_as_ushort(__float2bfloat16(lo));
    uint32_t b = (uint32_t)__bfloat16_as_ushort(__float2bfloat16(hi));
    return a | (b << 16);
}
__device__ __forceinline__ float silu_f(float v) { return v / (1.f + __expf(-v)); }
__device__ __forceinline__ float sigm_f(float v) { return 1.f / (1.f + __expf(-v)); }

#define SPIN_CAP (1u << 20)

#define STAMP(k) do { if (t == 0) stamps[wg * 16 + (k)] = __builtin_amdgcn_s_memrealtime(); } while (0)

__global__ void __launch_bounds__(NT, 3) se_fused(const uint4* __restrict__ x, const __hip_bfloat16* __restrict__ wd,
                                                  const __hip_bfloat16* __restrict__ wu, uint4* __restrict__ out,
                                                  unsigned long long* ws) {
    __shared__ float red[64 * C];
    __shared__ float red2[8 * C];
    __shared__ float s_sh[C];
    __shared__ float g_sh[C];
    __shared__ float h_sh[8];
    __shared__ float wd_sh[C * R];
    __shared__ float wu_sh[C * R];
    const int t = threadIdx.x;
    const int wg = blockIdx.x;
    const size_t base = (size_t)wg * (NT * PER);
    gu64* gran1 = (gu64*)ws;
    gu64* gran2 = gran1 + C * NWG;
    unsigned long long* stamps = ws + (1 << 20);
    STAMP(0);

    uint4 v[PER];
#pragma unroll
    for (int i = 0; i < PER; ++i) { v4u q = __builtin_nontemporal_load((const v4u*)&x[base + i * NT + t]); v[i] = make_uint4(q.x, q.y, q.z, q.w); }
    if (t < C * R) {
        wd_sh[t] = __bfloat162float(wd[t]);
        wu_sh[t] = __bfloat162float(wu[t]);
    }
    float a[8];
#pragma unroll
    for (int j = 0; j < 8; ++j) a[j] = 0.f;
#pragma unroll
    for (int i = 0; i < PER; ++i) {
        a[0] += bf_lo(v[i].x); a[1] += bf_hi(v[i].x);
        a[2] += bf_lo(v[i].y); a[3] += bf_hi(v[i].y);
        a[4] += bf_lo(v[i].z); a[5] += bf_hi(v[i].z);
        a[6] += bf_lo(v[i].w); a[7] += bf_hi(v[i].w);
    }
#pragma unroll
    for (int i = 0; i < PER; ++i) asm volatile("" : "+v"(v[i].x), "+v"(v[i].y), "+v"(v[i].z), "+v"(v[i].w));
#pragma unroll
    for (int j = 0; j < 8; ++j) red[t * 8 + j] = a[j];
    STAMP(1);
    __syncthreads();
    {
        const int c = t % C, k = t / C;
        float s = 0.f;
#pragma unroll
        for (int j = 0; j < 8; ++j) s += red[(k * 8 + j) * C + c];
        red2[k * C + c] = s;
    }
    __syncthreads();
    if (t < C) {
        float s = 0.f;
#pragma unroll
        for (int j = 0; j < 8; ++j) s += red2[j * C + t];
        __hip_atomic_store(gran1 + t * NWG + wg, (1ull << 32) | (unsigned long long)__float_as_uint(s), RLX_AGENT);
    }
    STAMP(2);
    if (wg < C && (t >> 6) == 2) {
        const int l = t & 63;
        gu64* gsrc = gran1 + wg * NWG + l * 4;
        float s;
        for (unsigned spins = 0;; ++spins) {
            unsigned long long q0 = __hip_atomic_load(gsrc + 0, RLX_AGENT);
            unsigned long long q1 = __hip_atomic_load(gsrc + 1, RLX_AGENT);
            unsigned long long q2 = __hip_atomic_load(gsrc + 2, RLX_AGENT);
            unsigned long long q3 = __hip_atomic_load(gsrc + 3, RLX_AGENT);
            bool ok = ((q0 >> 32) == 1ull) & ((q1 >> 32) == 1ull) & ((q2 >> 32) == 1ull) & ((q3 >> 32) == 1ull);
            s = (__uint_as_float((unsigned)q0) + __uint_as_float((unsigned)q1)) +
                (__uint_as_float((unsigned)q2) + __uint_as_float((unsigned)q3));
            if (__all(ok) || spins > SPIN_CAP) break;
            __builtin_amdgcn_s_sleep(1);
        }
#pragma unroll
        for (int m = 32; m >= 1; m >>= 1) s += __shfl_xor(s, m, 64);
        if (l == 0) __hip_atomic_store(gran2 + wg, (1ull << 32) | (unsigned long long)__float_as_uint(s), RLX_AGENT);
    }
    if (t < C) {
        unsigned long long q;
        for (unsigned spins = 0;; ++spins) {
            q = __hip_atomic_load(gran2 + t, RLX_AGENT);
            if ((q >> 32) == 1ull || spins > SPIN_CAP) break;
            __builtin_amdgcn_s_sleep(1);
        }
        s_sh[t] = rnd_bf16(__uint_as_float((unsigned)q) * (1.0f / NPIX));
    }
    __syncthreads();
    STAMP(3);
    if (t < R * 64) {
        const int r = t >> 6, l = t & 63;
        float p = s_sh[l] * wd_sh[l * R + r];
        if (l < C - 64) p += s_sh[l + 64] * wd_sh[(l + 64) * R + r];
#pragma unroll
        for (int m = 32; m >= 1; m >>= 1) p += __shfl_xor(p, m, 64);
        if (l == 0) h_sh[r] = rnd_bf16(silu_f(rnd_bf16(p)));
    }
    __syncthreads();
    if (t < C) {
        float gsum = 0.f;
#pragma unroll
        for (int r = 0; r < R; ++r) gsum += h_sh[r] * wu_sh[r * C + t];
        g_sh[t] = rnd_bf16(sigm_f(rnd_bf16(gsum)));
    }
    __syncthreads();
    STAMP(4);
    float gg[8];
#pragma unroll
    for (int j = 0; j < 8; ++j) gg[j] = g_sh[(t % 12) * 8 + j];
#pragma unroll
    for (int i = 0; i < PER; ++i) {
        uint4 o;
        o.x = pack_bf16(bf_lo(v[i].x) * gg[0], bf_hi(v[i].x) * gg[1]);
        o.y = pack_bf16(bf_lo(v[i].y) * gg[2], bf_hi(v[i].y) * gg[3]);
        o.z = pack_bf16(bf_lo(v[i].z) * gg[4], bf_hi(v[i].z) * gg[5]);
        o.w = pack_bf16(bf_lo(v[i].w) * gg[6], bf_hi(v[i].w) * gg[7]);
        { v4u q = {o.x, o.y, o.z, o.w}; __builtin_nontemporal_store(q, (v4u*)&out[base + i * NT + t]); }
    }
    STAMP(5);
    asm volatile("s_waitcnt vmcnt(0)" ::: "memory");
    STAMP(6);
}

__global__ void dump_stamps(unsigned long long* ws, const void* x, const void* out) {
    unsigned long long* st = ws + (1 << 20);
    unsigned long long t0 = ~0ull;
    for (int w = 0; w < 256; ++w) t0 = st[w * 16] < t0 ? st[w * 16] : t0;
    printf("x=%p out=%p ws=%p\n", x, out, (void*)ws);
    for (int k = 0; k < 7; ++k) {
        unsigned long long mn = ~0ull, mx = 0, sm = 0;
        for (int w = 0; w < 256; ++w) { unsigned long long d = st[w * 16 + k] - t0; mn = d < mn ? d : mn; mx = d > mx ? d : mx; sm += d; }
        printf("stamp %d: min %llu avg %llu max %llu (x10ns)\n", k, mn, sm / 256, mx);
    }
}

extern "C" void kernel_launch(void* const* d_in, const int* in_sizes, int n_in,
                              void* d_out, int out_size, void* d_ws, size_t ws_size,
                              hipStream_t stream) {
    const uint4* x = (const uint4*)d_in[0];
    const __hip_bfloat16* wd = (const __hip_bfloat16*)d_in[1];
    const __hip_bfloat16* wu = (const __hip_bfloat16*)d_in[2];
    (void)hipMemsetAsync(d_ws, 0, (size_t)(C * NWG + C) * 8, stream);
    se_fused<<<NWG, NT, 0, stream>>>(x, wd, wu, (uint4*)d_out, (unsigned long long*)d_ws);
    dump_stamps<<<1, 1, 0, stream>>>((unsigned long long*)d_ws, x, d_out);
}
```

```cpp
#include <hip/hip_runtime.h>
#include <hip/hip_bf16.h>
#include <stdint.h>

#define C 96
#define R 6
#define NPIX (512*512)
#define NWG 256
#define NT 768
#define PER 16

typedef uint32_t v4u __attribute__((ext_vector_type(4)));
#define GAS __attribute__((address_space(1)))
typedef GAS unsigned gu32;
#define RLX_AGENT __ATOMIC_RELAXED, __HIP_MEMORY_SCOPE_AGENT

__device__ __forceinline__ float bf_lo(uint32_t w) { return __uint_as_float(w << 16); }
__device__ __forceinline__ float bf_hi(uint32_t w) { return __uint_as_float(w & 0xffff0000u); }
__device__ __forceinline__ float rnd_bf16(float f) { return __bfloat162float(__float2bfloat16(f)); }
__device__ __forceinline__ uint32_t pack_bf16(float lo, float hi) {
    uint32_t a = (uint32_t)__bfloat16_as_ushort(__float2bfloat16(lo));
    uint32_t b = (uint32_t)__bfloat16_as_ushort(__float2bfloat16(hi));
    return a | (b << 16);
}
__device__ __forceinline__ float silu_f(float v) { return v / (1.f + __expf(-v)); }
__device__ __forceinline__ float sigm_f(float v) { return 1.f / (1.f + __expf(-v)); }

#define SPIN_CAP (1u << 20)
#define GKEY 0x9E3779B9u
#define G1_OFF 4096
#define G2_OFF (G1_OFF + C * NWG * 16)
#define WS_BYTES (G2_OFF + C * 16)

#define STAMP(k)

__device__ __forceinline__ v4u mk_gran(float v, unsigned e) {
    unsigned b = __float_as_uint(v);
    v4u g; g.x = b; g.y = e; g.z = b ^ e ^ GKEY; g.w = ~e; return g;
}
__device__ __forceinline__ bool gran_ok(v4u g, unsigned e) {
    return (g.y == e) & (g.w == ~e) & (g.z == (g.x ^ e ^ GKEY));
}

__global__ void __launch_bounds__(NT, 3) se_fused(const v4u* __restrict__ x, const __hip_bfloat16* __restrict__ wd,
                                                  const __hip_bfloat16* __restrict__ wu, v4u* __restrict__ out,
                                                  unsigned long long* ws) {
    __shared__ float red[64 * C];
    __shared__ float red2[8 * C];
    __shared__ float s_sh[C];
    __shared__ float g_sh[C];
    __shared__ float h_sh[8];
    __shared__ float wd_sh[C * R];
    __shared__ float wu_sh[C * R];
    const int t = threadIdx.x;
    const int wg = blockIdx.x;
    const size_t base = (size_t)wg * (NT * PER);
    const __amdgpu_buffer_rsrc_t rs = __builtin_amdgcn_make_buffer_rsrc((void*)ws, 0, WS_BYTES, 0x00020000);
    STAMP(0);
    const unsigned e = __hip_atomic_load((gu32*)ws, RLX_AGENT) + 1u;

    v4u v[PER];
#pragma unroll
    for (int i = 0; i < PER; ++i) v[i] = __builtin_nontemporal_load(&x[base + i * NT + t]);
    if (t < C * R) {
        wd_sh[t] = __bfloat162float(wd[t]);
        wu_sh[t] = __bfloat162float(wu[t]);
    }
    float a[8];
#pragma unroll
    for (int j = 0; j < 8; ++j) a[j] = 0.f;
#pragma unroll
    for (int i = 0; i < PER; ++i) {
        a[0] += bf_lo(v[i].x); a[1] += bf_hi(v[i].x);
        a[2] += bf_lo(v[i].y); a[3] += bf_hi(v[i].y);
        a[4] += bf_lo(v[i].z); a[5] += bf_hi(v[i].z);
        a[6] += bf_lo(v[i].w); a[7] += bf_hi(v[i].w);
    }
#pragma unroll
    for (int i = 0; i < PER; ++i) asm volatile("" : "+v"(v[i]));
#pragma unroll
    for (int j = 0; j < 8; ++j) red[t * 8 + j] = a[j];
    STAMP(1);
    __syncthreads();
    {
        const int c = t % C, k = t / C;
        float s = 0.f;
#pragma unroll
        for (int j = 0; j < 8; ++j) s += red[(k * 8 + j) * C + c];
        red2[k * C + c] = s;
    }
    __syncthreads();
    if (t < C) {
        float s = 0.f;
#pragma unroll
        for (int j = 0; j < 8; ++j) s += red2[j * C + t];
        __builtin_amdgcn_raw_buffer_store_b128(mk_gran(s, e), rs, G1_OFF + (t * NWG + wg) * 16, 0, 16);
    }
    STAMP(2);
    if (wg < C && (t >> 6) == 2) {
        const int l = t & 63;
        const int off = G1_OFF + (wg * NWG + l * 4) * 16;
        float s;
        for (unsigned spins = 0;; ++spins) {
            v4u q0 = __builtin_amdgcn_raw_buffer_load_b128(rs, off, 0, 16);
            v4u q1 = __builtin_amdgcn_raw_buffer_load_b128(rs, off + 16, 0, 16);
            v4u q2 = __builtin_amdgcn_raw_buffer_load_b128(rs, off + 32, 0, 16);
            v4u q3 = __builtin_amdgcn_raw_buffer_load_b128(rs, off + 48, 0, 16);
            bool ok = gran_ok(q0, e) & gran_ok(q1, e) & gran_ok(q2, e) & gran_ok(q3, e);
            s = (__uint_as_float(q0.x) + __uint_as_float(q1.x)) + (__uint_as_float(q2.x) + __uint_as_float(q3.x));
            if (__all(ok) || spins > SPIN_CAP) break;
            __builtin_amdgcn_s_sleep(1);
            asm volatile("" ::: "memory");
        }
#pragma unroll
        for (int m = 32; m >= 1; m >>= 1) s += __shfl_xor(s, m, 64);
        if (l == 0) __builtin_amdgcn_raw_buffer_store_b128(mk_gran(s, e), rs, G2_OFF + wg * 16, 0, 16);
    }
    if (t < C) {
        v4u q;
        for (unsigned spins = 0;; ++spins) {
            q = __builtin_amdgcn_raw_buffer_load_b128(rs, G2_OFF + t * 16, 0, 16);
            if (gran_ok(q, e) || spins > SPIN_CAP) break;
            __builtin_amdgcn_s_sleep(1);
            asm volatile("" ::: "memory");
        }
        s_sh[t] = rnd_bf16(__uint_as_float(q.x) * (1.0f / NPIX));
    }
    __syncthreads();
    STAMP(3);
    if (wg == 0 && t == 0) __hip_atomic_store((gu32*)ws, e, RLX_AGENT);
    if (t < R * 64) {
        const int r = t >> 6, l = t & 63;
        float p = s_sh[l] * wd_sh[l * R + r];
        if (l < C - 64) p += s_sh[l + 64] * wd_sh[(l + 64) * R + r];
#pragma unroll
        for (int m = 32; m >= 1; m >>= 1) p += __shfl_xor(p, m, 64);
        if (l == 0) h_sh[r] = rnd_bf16(silu_f(rnd_bf16(p)));
    }
    __syncthreads();
    if (t < C) {
        float gsum = 0.f;
#pragma unroll
        for (int r = 0; r < R; ++r) gsum += h_sh[r] * wu_sh[r * C + t];
        g_sh[t] = rnd_bf16(sigm_f(rnd_bf16(gsum)));
    }
    __syncthreads();
    STAMP(4);
    float gg[8];
#pragma unroll
    for (int j = 0; j < 8; ++j) gg[j] = g_sh[(t % 12) * 8 + j];
#pragma unroll
    for (int i = 0; i < PER; ++i) {
        v4u o;
        o.x = pack_bf16(bf_lo(v[i].x) * gg[0], bf_hi(v[i].x) * gg[1]);
        o.y = pack_bf16(bf_lo(v[i].y) * gg[2], bf_hi(v[i].y) * gg[3]);
        o.z = pack_bf16(bf_lo(v[i].z) * gg[4], bf_hi(v[i].z) * gg[5]);
        o.w = pack_bf16(bf_lo(v[i].w) * gg[6], bf_hi(v[i].w) * gg[7]);
        __builtin_nontemporal_store(o, &out[base + i * NT + t]);
    }
    STAMP(5);
}

extern "C" void kernel_launch(void* const* d_in, const int* in_sizes, int n_in,
                              void* d_out, int out_size, void* d_ws, size_t ws_size,
                              hipStream_t stream) {
    const v4u* x = (const v4u*)d_in[0];
    const __hip_bfloat16* wd = (const __hip_bfloat16*)d_in[1];
    const __hip_bfloat16* wu = (const __hip_bfloat16*)d_in[2];
    se_fused<<<NWG, NT, 0, stream>>>(x, wd, wu, (v4u*)d_out, (unsigned long long*)d_ws);
}
```

```cpp
#define LDNT 1
#define STNT 1
#define MAP 1
#include <hip/hip_runtime.h>
#include <hip/hip_bf16.h>
#include <stdint.h>

#define C 96
#define R 6
#define NPIX (512*512)
#define NWG 256
#define NT 768
#define PER 16

typedef uint32_t v4u __attribute__((ext_vector_type(4)));
#define GAS __attribute__((address_space(1)))
typedef GAS unsigned gu32;
#define RLX_AGENT __ATOMIC_RELAXED, __HIP_MEMORY_SCOPE_AGENT

__device__ __forceinline__ float bf_lo(uint32_t w) { return __uint_as_float(w << 16); }
__device__ __forceinline__ float bf_hi(uint32_t w) { return __uint_as_float(w & 0xffff0000u); }
__device__ __forceinline__ float rnd_bf16(float f) { return __bfloat162float(__float2bfloat16(f)); }
__device__ __forceinline__ uint32_t pack_bf16(float lo, float hi) {
    uint32_t a = (uint32_t)__bfloat16_as_ushort(__float2bfloat16(lo));
    uint32_t b = (uint32_t)__bfloat16_as_ushort(__float2bfloat16(hi));
    return a | (b << 16);
}
__device__ __forceinline__ float silu_f(float v) { return v / (1.f + __expf(-v)); }
__device__ __forceinline__ float sigm_f(float v) { return 1.f / (1.f + __expf(-v)); }

#define IDX(i) (((size_t)(i) * NWG + wg) * NT + t)
#define SPIN_CAP (1u << 20)
#define GKEY 0x9E3779B9u
#define G1_OFF 4096
#define G2_OFF (G1_OFF + C * NWG * 16)
#define WS_BYTES (G2_OFF + C * 16)

#define STAMP(k)

__device__ __forceinline__ v4u mk_gran(float v, unsigned e) {
    unsigned b = __float_as_uint(v);
    v4u g; g.x = b; g.y = e; g.z = b ^ e ^ GKEY; g.w = ~e; return g;
}
__device__ __forceinline__ bool gran_ok(v4u g, unsigned e) {
    return (g.y == e) & (g.w == ~e) & (g.z == (g.x ^ e ^ GKEY));
}

__global__ void __launch_bounds__(NT, 3) se_fused(const v4u* __restrict__ x, const __hip_bfloat16* __restrict__ wd,
                                                  const __hip_bfloat16* __restrict__ wu, v4u* __restrict__ out,
                                                  unsigned long long* ws) {
    __shared__ float red[64 * C];
    __shared__ float red2[8 * C];
    __shared__ float s_sh[C];
    __shared__ float g_sh[C];
    __shared__ float h_sh[8];
    __shared__ float wd_sh[C * R];
    __shared__ float wu_sh[C * R];
    const int t = threadIdx.x;
    const int wg = blockIdx.x;
    const size_t base = (size_t)wg * (NT * PER);
    const __amdgpu_buffer_rsrc_t rs = __builtin_amdgcn_make_buffer_rsrc((void*)ws, 0, WS_BYTES, 0x00020000);
    STAMP(0);
    const unsigned e = __hip_atomic_load((gu32*)ws, RLX_AGENT) + 1u;

    v4u v[PER];
#pragma unroll
    for (int i = 0; i < PER; ++i) v[i] = LDNT ? __builtin_nontemporal_load(&x[IDX(i)]) : x[IDX(i)];
    if (t < C * R) {
        wd_sh[t] = __bfloat162float(wd[t]);
        wu_sh[t] = __bfloat162float(wu[t]);
    }
    float a[8];
#pragma unroll
    for (int j = 0; j < 8; ++j) a[j] = 0.f;
#pragma unroll
    for (int i = 0; i < PER; ++i) {
        a[0] += bf_lo(v[i].x); a[1] += bf_hi(v[i].x);
        a[2] += bf_lo(v[i].y); a[3] += bf_hi(v[i].y);
        a[4] += bf_lo(v[i].z); a[5] += bf_hi(v[i].z);
        a[6] += bf_lo(v[i].w); a[7] += bf_hi(v[i].w);
    }
#pragma unroll
    for (int i = 0; i < PER; ++i) asm volatile("" : "+v"(v[i]));
#pragma unroll
    for (int j = 0; j < 8; ++j) red[t * 8 + j] = a[j];
    STAMP(1);
    __syncthreads();
    {
        const int c = t % C, k = t / C;
        float s = 0.f;
#pragma unroll
        for (int j = 0; j < 8; ++j) s += red[(k * 8 + j) * C + c];
        red2[k * C + c] = s;
    }
    __syncthreads();
    if (t < C) {
        float s = 0.f;
#pragma unroll
        for (int j = 0; j < 8; ++j) s += red2[j * C + t];
        __builtin_amdgcn_raw_buffer_store_b128(mk_gran(s, e), rs, G1_OFF + (t * NWG + wg) * 16, 0, 16);
    }
    STAMP(2);
    if (wg < C && (t >> 6) == 2) {
        const int l = t & 63;
        const int off = G1_OFF + (wg * NWG + l * 4) * 16;
        float s;
        for (unsigned spins = 0;; ++spins) {
            v4u q0 = __builtin_amdgcn_raw_buffer_load_b128(rs, off, 0, 16);
            v4u q1 = __builtin_amdgcn_raw_buffer_load_b128(rs, off + 16, 0, 16);
            v4u q2 = __builtin_amdgcn_raw_buffer_load_b128(rs, off + 32, 0, 16);
            v4u q3 = __builtin_amdgcn_raw_buffer_load_b128(rs, off + 48, 0, 16);
            bool ok = gran_ok(q0, e) & gran_ok(q1, e) & gran_ok(q2, e) & gran_ok(q3, e);
            s = (__uint_as_float(q0.x) + __uint_as_float(q1.x)) + (__uint_as_float(q2.x) + __uint_as_float(q3.x));
            if (__all(ok) || spins > SPIN_CAP) break;
            __builtin_amdgcn_s_sleep(1);
            asm volatile("" ::: "memory");
        }
#pragma unroll
        for (int m = 32; m >= 1; m >>= 1) s += __shfl_xor(s, m, 64);
        if (l == 0) __builtin_amdgcn_raw_buffer_store_b128(mk_gran(s, e), rs, G2_OFF + wg * 16, 0, 16);
    }
    if (t < C) {
        v4u q;
        for (unsigned spins = 0;; ++spins) {
            q = __builtin_amdgcn_raw_buffer_load_b128(rs, G2_OFF + t * 16, 0, 16);
            if (gran_ok(q, e) || spins > SPIN_CAP) break;
            __builtin_amdgcn_s_sleep(1);
            asm volatile("" ::: "memory");
        }
        s_sh[t] = rnd_bf16(__uint_as_float(q.x) * (1.0f / NPIX));
    }
    __syncthreads();
    STAMP(3);
    if (wg == 0 && t == 0) __hip_atomic_store((gu32*)ws, e, RLX_AGENT);
    if (t < R * 64) {
        const int r = t >> 6, l = t & 63;
        float p = s_sh[l] * wd_sh[l * R + r];
        if (l < C - 64) p += s_sh[l + 64] * wd_sh[(l + 64) * R + r];
#pragma unroll
        for (int m = 32; m >= 1; m >>= 1) p += __shfl_xor(p, m, 64);
        if (l == 0) h_sh[r] = rnd_bf16(silu_f(rnd_bf16(p)));
    }
    __syncthreads();
    if (t < C) {
        float gsum = 0.f;
#pragma unroll
        for (int r = 0; r < R; ++r) gsum += h_sh[r] * wu_sh[r * C + t];
        g_sh[t] = rnd_bf16(sigm_f(rnd_bf16(gsum)));
    }
    __syncthreads();
    STAMP(4);
    float gg[8];
#pragma unroll
    for (int j = 0; j < 8; ++j) gg[j] = g_sh[(t % 12) * 8 + j];
#pragma unroll
    for (int i = 0; i < PER; ++i) {
        v4u o;
        o.x = pack_bf16(bf_lo(v[i].x) * gg[0], bf_hi(v[i].x) * gg[1]);
        o.y = pack_bf16(bf_lo(v[i].y) * gg[2], bf_hi(v[i].y) * gg[3]);
        o.z = pack_bf16(bf_lo(v[i].z) * gg[4], bf_hi(v[i].z) * gg[5]);
        o.w = pack_bf16(bf_lo(v[i].w) * gg[6], bf_hi(v[i].w) * gg[7]);
        if (STNT) __builtin_nontemporal_store(o, &out[IDX(i)]); else out[IDX(i)] = o;
    }
    STAMP(5);
}

extern "C" void kernel_launch(void* const* d_in, const int* in_sizes, int n_in,
                              void* d_out, int out_size, void* d_ws, size_t ws_size,
                              hipStream_t stream) {
    const v4u* x = (const v4u*)d_in[0];
    const __hip_bfloat16* wd = (const __hip_bfloat16*)d_in[1];
    const __hip_bfloat16* wu = (const __hip_bfloat16*)d_in[2];
    se_fused<<<NWG, NT, 0, stream>>>(x, wd, wu, (v4u*)d_out, (unsigned long long*)d_ws);
}
```

```cpp
#include <hip/hip_runtime.h>
#include <hip/hip_bf16.h>
#include <stdint.h>

#define C 96
#define R 6
#define NPIX (512*512)
#define NWG 256
#define NT 768
#define PER 16

typedef uint32_t v4u __attribute__((ext_vector_type(4)));
#define GAS __attribute__((address_space(1)))
typedef GAS unsigned gu32;
#define RLX_AGENT __ATOMIC_RELAXED, __HIP_MEMORY_SCOPE_AGENT

__device__ __forceinline__ float bf_lo(uint32_t w) { return __uint_as_float(w << 16); }
__device__ __forceinline__ float bf_hi(uint32_t w) { return __uint_as_float(w & 0xffff0000u); }
__device__ __forceinline__ float rnd_bf16(float f) { return __bfloat162float(__float2bfloat16(f)); }
__device__ __forceinline__ uint32_t pack_bf16(float lo, float hi) {
    uint32_t a = (uint32_t)__bfloat16_as_ushort(__float2bfloat16(lo));
    uint32_t b = (uint32_t)__bfloat16_as_ushort(__float2bfloat16(hi));
    return a | (b << 16);
}
__device__ __forceinline__ float silu_f(float v) { return v / (1.f + __expf(-v)); }
__device__ __forceinline__ float sigm_f(float v) { return 1.f / (1.f + __expf(-v)); }

#define IDX(i) (((size_t)(i) * NWG + wg) * NT + t)
#define SPIN_CAP (1u << 20)
#define GKEY 0x9E3779B9u
#define G1_OFF 4096
#define G2_OFF (G1_OFF + C * NWG * 16)
#define WS_BYTES (G2_OFF + C * 16)


__device__ __forceinline__ v4u mk_gran(float v, unsigned e) {
    unsigned b = __float_as_uint(v);
    v4u g; g.x = b; g.y = e; g.z = b ^ e ^ GKEY; g.w = ~e; return g;
}
__device__ __forceinline__ bool gran_ok(v4u g, unsigned e) {
    return (g.y == e) & (g.w == ~e) & (g.z == (g.x ^ e ^ GKEY));
}

__global__ void __launch_bounds__(NT, 3) se_fused(const v4u* __restrict__ x, const __hip_bfloat16* __restrict__ wd,
                                                  const __hip_bfloat16* __restrict__ wu, v4u* __restrict__ out,
                                                  unsigned long long* ws) {
    __shared__ float red[64 * C];
    __shared__ float red2[8 * C];
    __shared__ float s_sh[C];
    __shared__ float g_sh[C];
    __shared__ float h_sh[8];
    __shared__ float wd_sh[C * R];
    __shared__ float wu_sh[C * R];
    const int t = threadIdx.x;
    const int wg = blockIdx.x;
    const __amdgpu_buffer_rsrc_t rs = __builtin_amdgcn_make_buffer_rsrc((void*)ws, 0, WS_BYTES, 0x00020000);
    const unsigned e = __hip_atomic_load((gu32*)ws, RLX_AGENT) + 1u;

    v4u v[PER];
#pragma unroll
    for (int i = 0; i < PER; ++i) v[i] = __builtin_nontemporal_load(&x[IDX(i)]);
    if (t < C * R) {
        wd_sh[t] = __bfloat162float(wd[t]);
        wu_sh[t] = __bfloat162float(wu[t]);
    }
    float a[8];
#pragma unroll
    for (int j = 0; j < 8; ++j) a[j] = 0.f;
#pragma unroll
    for (int i = 0; i < PER; ++i) {
        a[0] += bf_lo(v[i].x); a[1] += bf_hi(v[i].x);
        a[2] += bf_lo(v[i].y); a[3] += bf_hi(v[i].y);
        a[4] += bf_lo(v[i].z); a[5] += bf_hi(v[i].z);
        a[6] += bf_lo(v[i].w); a[7] += bf_hi(v[i].w);
    }
#pragma unroll
    for (int i = 0; i < PER; ++i) asm volatile("" : "+v"(v[i]));
#pragma unroll
    for (int j = 0; j < 8; ++j) red[t * 8 + j] = a[j];
    __syncthreads();
    {
        const int c = t % C, k = t / C;
        float s = 0.f;
#pragma unroll
        for (int j = 0; j < 8; ++j) s += red[(k * 8 + j) * C + c];
        red2[k * C + c] = s;
    }
    __syncthreads();
    if (t < C) {
        float s = 0.f;
#pragma unroll
        for (int j = 0; j < 8; ++j) s += red2[j * C + t];
        __builtin_amdgcn_raw_buffer_store_b128(mk_gran(s, e), rs, G1_OFF + (t * NWG + wg) * 16, 0, 16);
    }
    if (wg < C && (t >> 6) == 2) {
        const int l = t & 63;
        const int off = G1_OFF + (wg * NWG + l * 4) * 16;
        float s;
        for (unsigned spins = 0;; ++spins) {
            v4u q0 = __builtin_amdgcn_raw_buffer_load_b128(rs, off, 0, 16);
            v4u q1 = __builtin_amdgcn_raw_buffer_load_b128(rs, off + 16, 0, 16);
            v4u q2 = __builtin_amdgcn_raw_buffer_load_b128(rs, off + 32, 0, 16);
            v4u q3 = __builtin_amdgcn_raw_buffer_load_b128(rs, off + 48, 0, 16);
            bool ok = gran_ok(q0, e) & gran_ok(q1, e) & gran_ok(q2, e) & gran_ok(q3, e);
            s = (__uint_as_float(q0.x) + __uint_as_float(q1.x)) + (__uint_as_float(q2.x) + __uint_as_float(q3.x));
            if (__all(ok) || spins > SPIN_CAP) break;
            __builtin_amdgcn_s_sleep(1);
            asm volatile("" ::: "memory");
        }
#pragma unroll
        for (int m = 32; m >= 1; m >>= 1) s += __shfl_xor(s, m, 64);
        if (l == 0) __builtin_amdgcn_raw_buffer_store_b128(mk_gran(s, e), rs, G2_OFF + wg * 16, 0, 16);
    }
    if (t < C) {
        v4u q;
        for (unsigned spins = 0;; ++spins) {
            q = __builtin_amdgcn_raw_buffer_load_b128(rs, G2_OFF + t * 16, 0, 16);
            if (gran_ok(q, e) || spins > SPIN_CAP) break;
            __builtin_amdgcn_s_sleep(1);
            asm volatile("" ::: "memory");
        }
        s_sh[t] = rnd_bf16(__uint_as_float(q.x) * (1.0f / NPIX));
    }
    __syncthreads();
    if (wg == 0 && t == 0) __hip_atomic_store((gu32*)ws, e, RLX_AGENT);
    if (t < R * 64) {
        const int r = t >> 6, l = t & 63;
        float p = s_sh[l] * wd_sh[l * R + r];
        if (l < C - 64) p += s_sh[l + 64] * wd_sh[(l + 64) * R + r];
#pragma unroll
        for (int m = 32; m >= 1; m >>= 1) p += __shfl_xor(p, m, 64);
        if (l == 0) h_sh[r] = rnd_bf16(silu_f(rnd_bf16(p)));
    }
    __syncthreads();
    if (t < C) {
        float gsum = 0.f;
#pragma unroll
        for (int r = 0; r < R; ++r) gsum += h_sh[r] * wu_sh[r * C + t];
        g_sh[t] = rnd_bf16(sigm_f(rnd_bf16(gsum)));
    }
    __syncthreads();
    float gg[8];
#pragma unroll
    for (int j = 0; j < 8; ++j) gg[j] = g_sh[(t % 12) * 8 + j];
#pragma unroll
    for (int i = 0; i < PER; ++i) {
        v4u o;
        o.x = pack_bf16(bf_lo(v[i].x) * gg[0], bf_hi(v[i].x) * gg[1]);
        o.y = pack_bf16(bf_lo(v[i].y) * gg[2], bf_hi(v[i].y) * gg[3]);
        o.z = pack_bf16(bf_lo(v[i].z) * gg[4], bf_hi(v[i].z) * gg[5]);
        o.w = pack_bf16(bf_lo(v[i].w) * gg[6], bf_hi(v[i].w) * gg[7]);
        __builtin_nontemporal_store(o, &out[IDX(i)]);
    }
}

extern "C" void kernel_launch(void* const* d_in, const int* in_sizes, int n_in,
                              void* d_out, int out_size, void* d_ws, size_t ws_size,
                              hipStream_t stream) {
    const v4u* x = (const v4u*)d_in[0];
    const __hip_bfloat16* wd = (const __hip_bfloat16*)d_in[1];
    const __hip_bfloat16* wu = (const __hip_bfloat16*)d_in[2];
    se_fused<<<NWG, NT, 0, stream>>>(x, wd, wu, (v4u*)d_out, (unsigned long long*)d_ws);
}
```
